```python
import torch

def _softmax(x, axis=-1):
    return torch.softmax(x, dim=axis)
def _gelu(x):
    return torch.nn.functional.gelu(x, approximate="tanh")
def _silu(x):
    return torch.nn.functional.silu(x)
def _sigmoid(x):
    return torch.sigmoid(x)
def _layernorm(x, axis=-1, eps=1e-5):
    m = x.mean(dim=axis, keepdim=True)
    v = x.var(dim=axis, keepdim=True, unbiased=False)
    return (x - m) / torch.sqrt(v + eps)
def _scatter_add(tgt, idx, src, axis=0):
    return tgt.clone().index_add_(axis, idx, src)
def _segment_sum(src, seg, n):
    out = torch.zeros((n,) + src.shape[1:], dtype=src.dtype)
    return out.index_add_(0, seg, src)
def _pad(x, pw):
    flat = tuple(v for pair in reversed(pw) for v in pair)
    return torch.nn.functional.pad(x, flat)

kernel_name = 'standalone__paged_block_table_kv_pagedattention__sampling'

def setup_inputs():

    _w = (lambda a: (lambda t: t.to(torch.bfloat16) if t.is_floating_point() else t)(torch.as_tensor(a)))
    B, Hq, Hkv, Sq, Skv, D = 32, 16, 8, 1, 2048, 256
    bs = 16
    nbps = max(1, Skv // bs)
    num_phys = B * nbps + 4
    bt = torch.randperm(num_phys)[: B * nbps].reshape(B, nbps).to(torch.int64)
    cl = (nbps * bs - (torch.arange(1, B + 1, dtype=torch.int64) * 2654435761) % max(1, nbps * bs // 2)).to(torch.int64)
    return {
        "q": _w((lambda *s: torch.randn(*s, dtype=torch.bfloat16) * 0.8)(B, Hq, Sq, D)),
        "k_cache": _w((lambda *s: torch.randn(*s, dtype=torch.bfloat16) * 0.8)(num_phys, bs, Hkv, D)),
        "v_cache": _w((lambda *s: torch.randn(*s, dtype=torch.bfloat16) * 0.05)(num_phys, bs, Hkv, D)),
        "block_table": _w(bt),
        "context_lens": _w(cl),
    }

def reference(q, k_cache, v_cache, block_table, context_lens):
    B, Hq, Sq, D = q.shape
    bs = k_cache.shape[1]
    Hkv = k_cache.shape[2]
    nbps = block_table.shape[1]
    Skv = nbps * bs
    bt = block_table.reshape(-1)
    k = (lambda x, *d: x.permute(*d))(k_cache[bt].reshape(B, Skv, Hkv, D), 0, 2, 1, 3)
    v = (lambda x, *d: x.permute(*d))(v_cache[bt].reshape(B, Skv, Hkv, D), 0, 2, 1, 3)
    g = Hq // Hkv
    qg = q.reshape(B, Hkv, g, Sq, D)
    scores = torch.einsum("bhgqd,bhkd->bhgqk", qg, k) * (D ** -0.5)
    pos = torch.arange(Skv)
    msk = pos[None, :] < context_lens[:, None]
    scores = torch.where(msk[:, None, None, None, :], scores, -1e9)
    p = _softmax(scores, axis=-1)
    out = torch.einsum("bhgqk,bhkd->bhgqd", p, v)
    return out.reshape(B, Hq, Sq, D)
```

```cpp
#include <hip/hip_runtime.h>
#include <hip/hip_bf16.h>
#include <cstdint>

#define NWAVES 16
#define DEPTH 1
#define KV_POL " nt"

constexpr int B_ = 32, HQ = 16, HKV = 8, D_ = 256, PAGE = 16, NBPS = 128;
constexpr int W = NWAVES;
constexpr int PAGE_BYTES = PAGE * HKV * D_ * 2;
constexpr int TOK_BYTES = HKV * D_ * 2;

using bf16x8 = __attribute__((ext_vector_type(8))) __bf16;
using f32x4 = __attribute__((ext_vector_type(4))) float;
using u32x4 = __attribute__((ext_vector_type(4))) unsigned;

template <int CTRL> __device__ __forceinline__ float dpp(float x) {
  return __builtin_bit_cast(float, __builtin_amdgcn_mov_dpp(__builtin_bit_cast(int, x), CTRL, 0xf, 0xf, true));
}
__device__ __forceinline__ float xrow_max(float x) {
  auto s = __builtin_amdgcn_permlane16_swap(__float_as_uint(x), __float_as_uint(x), false, false);
  x = fmaxf(__uint_as_float(s[0]), __uint_as_float(s[1]));
  auto t = __builtin_amdgcn_permlane32_swap(__float_as_uint(x), __float_as_uint(x), false, false);
  return fmaxf(__uint_as_float(t[0]), __uint_as_float(t[1]));
}
__device__ __forceinline__ float xrow_sum(float x) {
  auto s = __builtin_amdgcn_permlane16_swap(__float_as_uint(x), __float_as_uint(x), false, false);
  x = __uint_as_float(s[0]) + __uint_as_float(s[1]);
  auto t = __builtin_amdgcn_permlane32_swap(__float_as_uint(x), __float_as_uint(x), false, false);
  return __uint_as_float(t[0]) + __uint_as_float(t[1]);
}
__device__ __forceinline__ unsigned cvtpk(float lo, float hi) {
  typedef __attribute__((ext_vector_type(2))) __bf16 bf16x2_t;
  typedef __attribute__((ext_vector_type(2))) float f32x2_t;
  return __builtin_bit_cast(unsigned, __builtin_convertvector(f32x2_t{lo, hi}, bf16x2_t));
}
__device__ __forceinline__ f32x4 mfma(u32x4 a, u32x4 b, f32x4 c) {
  return __builtin_amdgcn_mfma_f32_16x16x32_bf16(__builtin_bit_cast(bf16x8, a), __builtin_bit_cast(bf16x8, b), c, 0, 0, 0);
}

struct Tile { u32x4 r[8]; };

__device__ __forceinline__ void issue_tile(Tile& T, const unsigned char* base, unsigned pk, int voff) {
  const uint64_t a = (uint64_t)base + ((uint64_t)(pk >> 5) << 16);
  u32x4 srd = {(unsigned)a, (unsigned)(a >> 32) & 0xffffu, (pk & 31u) << 12, 0x00020000u};
  asm volatile(
      "s_nop 4\n\t"
      "buffer_load_dwordx4 %0, %8, %9, 0 offen" KV_POL "\n\t"
      "buffer_load_dwordx4 %1, %8, %9, 0 offen offset:256" KV_POL "\n\t"
      "buffer_load_dwordx4 %2, %8, %9, %10 offen" KV_POL "\n\t"
      "buffer_load_dwordx4 %3, %8, %9, %10 offen offset:256" KV_POL "\n\t"
      "buffer_load_dwordx4 %4, %8, %9, %11 offen" KV_POL "\n\t"
      "buffer_load_dwordx4 %5, %8, %9, %11 offen offset:256" KV_POL "\n\t"
      "buffer_load_dwordx4 %6, %8, %9, %12 offen" KV_POL "\n\t"
      "buffer_load_dwordx4 %7, %8, %9, %12 offen offset:256" KV_POL
      : "+v"(T.r[0]), "+v"(T.r[1]), "+v"(T.r[2]), "+v"(T.r[3]), "+v"(T.r[4]), "+v"(T.r[5]), "+v"(T.r[6]), "+v"(T.r[7])
      : "v"(voff), "s"(srd), "s"(4 * TOK_BYTES), "s"(8 * TOK_BYTES), "s"(12 * TOK_BYTES));
}
__device__ __forceinline__ void issue_tile_first(Tile& T, const unsigned char* base, unsigned pk, int voff) {
  const uint64_t a = (uint64_t)base + ((uint64_t)(pk >> 5) << 16);
  u32x4 srd = {(unsigned)a, (unsigned)(a >> 32) & 0xffffu, (pk & 31u) << 12, 0x00020000u};
  asm volatile(
      "s_nop 4\n\t"
      "buffer_load_dwordx4 %0, %8, %9, 0 offen" KV_POL "\n\t"
      "buffer_load_dwordx4 %1, %8, %9, 0 offen offset:256" KV_POL "\n\t"
      "buffer_load_dwordx4 %2, %8, %9, %10 offen" KV_POL "\n\t"
      "buffer_load_dwordx4 %3, %8, %9, %10 offen offset:256" KV_POL "\n\t"
      "buffer_load_dwordx4 %4, %8, %9, %11 offen" KV_POL "\n\t"
      "buffer_load_dwordx4 %5, %8, %9, %11 offen offset:256" KV_POL "\n\t"
      "buffer_load_dwordx4 %6, %8, %9, %12 offen" KV_POL "\n\t"
      "buffer_load_dwordx4 %7, %8, %9, %12 offen offset:256" KV_POL
      : "=&v"(T.r[0]), "=&v"(T.r[1]), "=&v"(T.r[2]), "=&v"(T.r[3]), "=&v"(T.r[4]), "=&v"(T.r[5]), "=&v"(T.r[6]), "=&v"(T.r[7])
      : "v"(voff), "s"(srd), "s"(4 * TOK_BYTES), "s"(8 * TOK_BYTES), "s"(12 * TOK_BYTES));
}
template <int N> __device__ __forceinline__ void wait_tile(Tile& T) {
  asm volatile("s_waitcnt vmcnt(%8)"
               : "+v"(T.r[0]), "+v"(T.r[1]), "+v"(T.r[2]), "+v"(T.r[3]), "+v"(T.r[4]), "+v"(T.r[5]), "+v"(T.r[6]), "+v"(T.r[7])
               : "i"(N));
}

__global__ void __launch_bounds__(W * 64, (W * 64) / 256)
attn_kernel(const unsigned short* __restrict__ q, const unsigned char* __restrict__ kc, const unsigned char* __restrict__ vc,
            const int* __restrict__ bt, const int* __restrict__ lens, unsigned short* __restrict__ out) {
  __shared__ __attribute__((aligned(16))) float lds[W * (2 * D_ + 4)];
  const int h = blockIdx.x & 7, b = blockIdx.x >> 3;
  const int tid = threadIdx.x, lane = tid & 63;
  const int wave = __builtin_amdgcn_readfirstlane(tid >> 6);
  const int G = lane >> 4, c = lane & 15;
  constexpr float C2 = 0.0625f * 1.4426950408889634f;

  u32x4 qf[2];
  {
    const unsigned short* qp = q + ((size_t)b * HQ + h * 2 + (c >> 3)) * D_ + (4 * G + (c & 3)) * 8;
    qf[0] = *reinterpret_cast<const u32x4*>(qp);
    qf[1] = *reinterpret_cast<const u32x4*>(qp + 128);
  }
  unsigned btv = bt[b * NBPS + min(wave + W * lane, NBPS - 1)];
  int len = lens[b];
  len = min(max(len, 1), NBPS * PAGE);
  const int npages = (len + PAGE - 1) / PAGE;
  const int nwp = (npages > wave) ? (npages - wave + W - 1) / W : 0;

  const int koff = ((lane >> 2) & 3) * TOK_BYTES + h * (D_ * 2) + (4 * G + (lane & 3)) * 16;
  const int voff = G * TOK_BYTES + h * (D_ * 2) + c * 16;
  unsigned pm[4];
#pragma unroll
  for (int i = 0; i < 4; ++i) pm[i] = (((lane & 7) >> 1) == i) ? ((lane & 1) ? 0xffff0000u : 0x0000ffffu) : 0u;

  float m = -INFINITY, l = 0.f;
  f32x4 acc0 = {0.f, 0.f, 0.f, 0.f}, acc1 = {0.f, 0.f, 0.f, 0.f};

  Tile K[DEPTH], V[DEPTH];

  btv = (btv << 5) | (unsigned)max(min(len - (wave + W * lane) * PAGE, PAGE), 0);
  asm volatile("" : "+v"(qf[0]), "+v"(qf[1]), "+v"(btv));

  auto compute = [&](Tile& Kt, Tile& Vt, int i) {
    constexpr int NW = 16 * DEPTH - 8;
    const unsigned pk_next = __builtin_amdgcn_readlane(btv, (i + DEPTH) & 63);
    const int rem = __builtin_amdgcn_readlane(btv, i) & 31;
    wait_tile<NW>(Kt);
    float s[4];
#pragma unroll
    for (int u = 0; u < 4; ++u) {
      f32x4 d = {0.f, 0.f, 0.f, 0.f};
      d = mfma(Kt.r[2 * u], qf[0], d);
      d = mfma(Kt.r[2 * u + 1], qf[1], d);
      s[u] = (dpp<0x00>(d[0]) + dpp<0x55>(d[1])) + (dpp<0xAA>(d[2]) + dpp<0xFF>(d[3]));
    }
    issue_tile(Kt, kc, pk_next, koff);
    if (rem < PAGE) {
#pragma unroll
      for (int u = 0; u < 4; ++u) s[u] = (4 * u + G < rem) ? s[u] : -INFINITY;
    }
    float mx = fmaxf(fmaxf(s[0], s[1]), fmaxf(s[2], s[3]));
    mx = xrow_max(mx) * C2;
    if (__any(mx > m)) {
      const float mn = fmaxf(m, mx);
      const float al = __builtin_amdgcn_exp2f(m - mn);
      m = mn;
      l *= al;
      const float a0 = __builtin_bit_cast(float, __builtin_amdgcn_readlane(__builtin_bit_cast(int, al), 0));
      const float a1 = __builtin_bit_cast(float, __builtin_amdgcn_readlane(__builtin_bit_cast(int, al), 8));
      const float aa = (lane >= 32) ? a1 : a0;
      acc0 *= aa; acc1 *= aa;
    }
    u32x4 a[4];
#pragma unroll
    for (int u = 0; u < 4; ++u) {
      const float p = __builtin_amdgcn_exp2f(fmaf(s[u], C2, -m));
      l += p;
      const unsigned pk = cvtpk(p, p);
      a[u] = u32x4{pk & pm[0], pk & pm[1], pk & pm[2], pk & pm[3]};
    }
    wait_tile<NW>(Vt);
#pragma unroll
    for (int u = 0; u < 4; ++u) {
      acc0 = mfma(a[u], Vt.r[2 * u], acc0);
      acc1 = mfma(a[u], Vt.r[2 * u + 1], acc1);
    }
    issue_tile(Vt, vc, pk_next, voff);
  };

#pragma unroll
  for (int j = 0; j < DEPTH; ++j) {
    const unsigned pk = __builtin_amdgcn_readlane(btv, j);
    issue_tile_first(K[j], kc, pk, koff);
    issue_tile_first(V[j], vc, pk, voff);
  }
  if (nwp > 0) {
    int i = 0;
    for (;;) {
      compute(K[0], V[0], i); if (++i >= nwp) break;
      if constexpr (DEPTH > 1) { compute(K[1 % DEPTH], V[1 % DEPTH], i); if (++i >= nwp) break; }
      if constexpr (DEPTH > 2) { compute(K[2 % DEPTH], V[2 % DEPTH], i); if (++i >= nwp) break; }
      if constexpr (DEPTH > 3) { compute(K[3 % DEPTH], V[3 % DEPTH], i); if (++i >= nwp) break; }
    }
  }
#pragma unroll
  for (int j = 0; j < DEPTH; ++j) { wait_tile<0>(K[j]); wait_tile<0>(V[j]); }

  l = xrow_sum(l);
  float* rec = lds + wave * (2 * D_ + 4);
  {
    const int qq = lane >> 5, d0 = 8 * c + 4 * (G & 1);
    *reinterpret_cast<f32x4*>(rec + 4 + qq * D_ + d0) = acc0;
    *reinterpret_cast<f32x4*>(rec + 4 + qq * D_ + 128 + d0) = acc1;
    if (lane == 0 || lane == 8) { rec[lane >> 3] = m; rec[2 + (lane >> 3)] = l; }
  }
  __syncthreads();
  if (tid < 256) {
    const int qq = tid >> 7, d0 = (tid & 127) * 2;
    float M = -INFINITY;
#pragma unroll
    for (int w = 0; w < W; ++w) M = fmaxf(M, lds[w * (2 * D_ + 4) + qq]);
    float L = 0.f, o0 = 0.f, o1 = 0.f;
#pragma unroll
    for (int w = 0; w < W; ++w) {
      const float* r = lds + w * (2 * D_ + 4);
      const float lw = r[2 + qq];
      const float wt = (lw > 0.f) ? __builtin_amdgcn_exp2f(r[qq] - M) : 0.f;
      const float2 a = *reinterpret_cast<const float2*>(r + 4 + qq * D_ + d0);
      L = fmaf(wt, lw, L); o0 = fmaf(wt, a.x, o0); o1 = fmaf(wt, a.y, o1);
    }
    const float inv = 1.f / L;
    *reinterpret_cast<unsigned*>(out + ((size_t)b * HQ + h * 2 + qq) * D_ + d0) = cvtpk(o0 * inv, o1 * inv);
  }
}

extern "C" void kernel_launch(void* const* d_in, const int* in_sizes, int n_in, void* d_out, int out_size, void* d_ws,
                              size_t ws_size, hipStream_t stream) {
  attn_kernel<<<B_ * HKV, W * 64, 0, stream>>>((const unsigned short*)d_in[0], (const unsigned char*)d_in[1],
                                               (const unsigned char*)d_in[2], (const int*)d_in[3], (const int*)d_in[4],
                                               (unsigned short*)d_out);
}
```

```cpp
#include <hip/hip_runtime.h>
#include <hip/hip_bf16.h>
#include <cstdint>
#include <type_traits>

constexpr int B_ = 32, HQ = 16, HKV = 8, D_ = 256, PAGE = 16, NBPS = 128;
constexpr int W = 4;
constexpr int TOK_BYTES = HKV * D_ * 2;
constexpr int SLOT_BYTES = 5 * 1024, NSLOT = 160;
constexpr int WS_SLOTS = 4096;
#define LDS_BYTES (84 * 1024)
constexpr float M_INIT = -3.0e38f;

using bf16x8 = __attribute__((ext_vector_type(8))) __bf16;
using f32x4 = __attribute__((ext_vector_type(4))) float;
using u32x4 = __attribute__((ext_vector_type(4))) unsigned;

template <int CTRL> __device__ __forceinline__ float dpp(float x) {
  return __builtin_bit_cast(float, __builtin_amdgcn_mov_dpp(__builtin_bit_cast(int, x), CTRL, 0xf, 0xf, true));
}
__device__ __forceinline__ float xrow_max(float x) {
  auto s = __builtin_amdgcn_permlane16_swap(__float_as_uint(x), __float_as_uint(x), false, false);
  x = fmaxf(__uint_as_float(s[0]), __uint_as_float(s[1]));
  auto t = __builtin_amdgcn_permlane32_swap(__float_as_uint(x), __float_as_uint(x), false, false);
  return fmaxf(__uint_as_float(t[0]), __uint_as_float(t[1]));
}
__device__ __forceinline__ float xrow_sum(float x) {
  auto s = __builtin_amdgcn_permlane16_swap(__float_as_uint(x), __float_as_uint(x), false, false);
  x = __uint_as_float(s[0]) + __uint_as_float(s[1]);
  auto t = __builtin_amdgcn_permlane32_swap(__float_as_uint(x), __float_as_uint(x), false, false);
  return __uint_as_float(t[0]) + __uint_as_float(t[1]);
}
__device__ __forceinline__ unsigned cvtpk(float lo, float hi) {
  typedef __attribute__((ext_vector_type(2))) __bf16 bf16x2_t;
  typedef __attribute__((ext_vector_type(2))) float f32x2_t;
  return __builtin_bit_cast(unsigned, __builtin_convertvector(f32x2_t{lo, hi}, bf16x2_t));
}
__device__ __forceinline__ f32x4 mfma(u32x4 a, u32x4 b, f32x4 c) {
  return __builtin_amdgcn_mfma_f32_16x16x32_bf16(__builtin_bit_cast(bf16x8, a), __builtin_bit_cast(bf16x8, b), c, 0, 0, 0);
}
__device__ __forceinline__ unsigned rdl(unsigned x, int l) { return (unsigned)__builtin_amdgcn_readlane((int)x, l); }
__device__ __forceinline__ unsigned scan32(unsigned x, int lane) {
  x += (unsigned)__builtin_amdgcn_update_dpp(0, (int)x, 0x111, 0xf, 0xf, false);
  x += (unsigned)__builtin_amdgcn_update_dpp(0, (int)x, 0x112, 0xf, 0xf, false);
  x += (unsigned)__builtin_amdgcn_update_dpp(0, (int)x, 0x114, 0xf, 0xf, false);
  x += (unsigned)__builtin_amdgcn_update_dpp(0, (int)x, 0x118, 0xf, 0xf, false);
  const unsigned t = rdl(x, 15);
  return (lane & 16) ? x + t : x;
}
__device__ __forceinline__ float to_acc_layout(float a, int lane) {
  const float a0 = __builtin_bit_cast(float, __builtin_amdgcn_readlane(__builtin_bit_cast(int, a), 0));
  const float a1 = __builtin_bit_cast(float, __builtin_amdgcn_readlane(__builtin_bit_cast(int, a), 8));
  return (lane >= 32) ? a1 : a0;
}

struct KTile { u32x4 r[8]; u32x4 q[2]; };
struct VTile { u32x4 r[8]; };
struct Imp { u32x4 r[5]; };

#define LOADS8(C)                                                      \
  "buffer_load_dwordx4 %0, %[vo], %[srd], 0 offen nt\n\t"              \
  "buffer_load_dwordx4 %1, %[vo], %[srd], 0 offen offset:256 nt\n\t"   \
  "buffer_load_dwordx4 %2, %[vo], %[srd], %[s1] offen nt\n\t"          \
  "buffer_load_dwordx4 %3, %[vo], %[srd], %[s1] offen offset:256 nt\n\t" \
  "buffer_load_dwordx4 %4, %[vo], %[srd], %[s2] offen nt\n\t"          \
  "buffer_load_dwordx4 %5, %[vo], %[srd], %[s2] offen offset:256 nt\n\t" \
  "buffer_load_dwordx4 %6, %[vo], %[srd], %[s3] offen nt\n\t"          \
  "buffer_load_dwordx4 %7, %[vo], %[srd], %[s3] offen offset:256 nt"

template <bool FIRST>
__device__ __forceinline__ void issue_k(KTile& T, const unsigned char* kc, u32x4 qsrd, unsigned d, int h, int koff, int qoff) {
  const uint64_t a = (uint64_t)kc + ((uint64_t)(d >> 11) << 16);
  const u32x4 srd = {(unsigned)a, (unsigned)(a >> 32) & 0xffffu, (d & 1024u) ? 0u : (d & 31u) << 12, 0x00020000u};
  const unsigned qs = (((d >> 5) & 31u) * HQ + 2 * h) * (D_ * 2) + ((d & 1024u) << 20);
#define KASM(CON)                                                                                                     \
  asm volatile("s_nop 4\n\t"                                                                                          \
               "buffer_load_dwordx4 %8, %[qo], %[qsrd], %[qs] offen\n\t"                                              \
               "buffer_load_dwordx4 %9, %[qo], %[qsrd], %[qs] offen offset:256\n\t" LOADS8(0)                         \
               : CON(T.r[0]), CON(T.r[1]), CON(T.r[2]), CON(T.r[3]), CON(T.r[4]), CON(T.r[5]), CON(T.r[6]), CON(T.r[7]), \
                 CON(T.q[0]), CON(T.q[1])                                                                             \
               : [vo] "v"(koff), [srd] "s"(srd), [s1] "s"(4 * TOK_BYTES), [s2] "s"(8 * TOK_BYTES),                    \
                 [s3] "s"(12 * TOK_BYTES), [qo] "v"(qoff), [qsrd] "s"(qsrd), [qs] "s"(qs)                             \
               : "memory")
  if constexpr (FIRST) KASM("=&v"); else KASM("+v");
#undef KASM
}
template <bool FIRST>
__device__ __forceinline__ void issue_v(VTile& T, const unsigned char* vc, unsigned d, int voff) {
  const uint64_t a = (uint64_t)vc + ((uint64_t)(d >> 11) << 16);
  const u32x4 srd = {(unsigned)a, (unsigned)(a >> 32) & 0xffffu, (d & 31u) << 12, 0x00020000u};
#define VASM(CON)                                                                                                     \
  asm volatile("s_nop 4\n\t" LOADS8(0)                                                                                \
               : CON(T.r[0]), CON(T.r[1]), CON(T.r[2]), CON(T.r[3]), CON(T.r[4]), CON(T.r[5]), CON(T.r[6]), CON(T.r[7]) \
               : [vo] "v"(voff), [srd] "s"(srd), [s1] "s"(4 * TOK_BYTES), [s2] "s"(8 * TOK_BYTES),                    \
                 [s3] "s"(12 * TOK_BYTES)                                                                             \
               : "memory")
  if constexpr (FIRST) VASM("=&v"); else VASM("+v");
#undef VASM
}
template <int N> __device__ __forceinline__ void wait_k(KTile& T) {
  asm volatile("s_waitcnt vmcnt(%10)"
               : "+v"(T.r[0]), "+v"(T.r[1]), "+v"(T.r[2]), "+v"(T.r[3]), "+v"(T.r[4]), "+v"(T.r[5]), "+v"(T.r[6]), "+v"(T.r[7]),
                 "+v"(T.q[0]), "+v"(T.q[1])
               : "i"(N));
}
template <int N> __device__ __forceinline__ void wait_v(VTile& T) {
  asm volatile("s_waitcnt vmcnt(%8)"
               : "+v"(T.r[0]), "+v"(T.r[1]), "+v"(T.r[2]), "+v"(T.r[3]), "+v"(T.r[4]), "+v"(T.r[5]), "+v"(T.r[6]), "+v"(T.r[7])
               : "i"(N));
}
__device__ __forceinline__ void issue_imp(Imp& I, u32x4 wsrd, unsigned soff, int loff) {
  asm volatile("s_nop 4\n\t"
               "buffer_load_dwordx4 %0, %5, %6, %7 offen sc1\n\t"
               "buffer_load_dwordx4 %1, %5, %6, %7 offen offset:1024 sc1\n\t"
               "buffer_load_dwordx4 %2, %5, %6, %7 offen offset:2048 sc1\n\t"
               "buffer_load_dwordx4 %3, %5, %6, %7 offen offset:3072 sc1\n\t"
               "buffer_load_dwordx4 %4, %5, %6, %8 offen sc1"
               : "=&v"(I.r[0]), "=&v"(I.r[1]), "=&v"(I.r[2]), "=&v"(I.r[3]), "=&v"(I.r[4])
               : "v"(loff), "s"(wsrd), "s"(soff), "s"(soff + 4096u)
               : "memory");
}
template <int N> __device__ __forceinline__ void wait_imp(Imp& I) {
  asm volatile("s_waitcnt vmcnt(%5)" : "+v"(I.r[0]), "+v"(I.r[1]), "+v"(I.r[2]), "+v"(I.r[3]), "+v"(I.r[4]) : "i"(N) : "memory");
}

__global__ void __launch_bounds__(W * 64, 2)
attn_kernel(const unsigned char* __restrict__ q, const unsigned char* __restrict__ kc, const unsigned char* __restrict__ vc,
            const int* __restrict__ bt, const int* __restrict__ lens, unsigned short* __restrict__ out, unsigned char* ws, unsigned kv_chunks
            ) {
#define STAMP(i)
  STAMP(0);
  extern __shared__ __attribute__((aligned(16))) float lds[];
  const int h = blockIdx.x & 7, b = blockIdx.x >> 3;
  const int tid = threadIdx.x, lane = tid & 63;
  const int wave = __builtin_amdgcn_readfirstlane(tid >> 6);
  const int G = lane >> 4, c = lane & 15;
  constexpr float C2 = 0.0625f * 1.4426950408889634f;

  int lenv; unsigned btv;
  asm volatile("global_load_dword %0, %2, %3\n\tglobal_load_dword %1, %4, %5"
               : "=&v"(lenv), "=&v"(btv)
               : "v"((lane & 31) * 4), "s"(lens), "v"((b * NBPS + wave + W * (lane & 1)) * 4), "s"(bt) : "memory");
  const int koff = ((lane >> 2) & 3) * TOK_BYTES + h * (D_ * 2) + (4 * G + (lane & 3)) * 16;
  const int voff = G * TOK_BYTES + h * (D_ * 2) + c * 16;
  const int qoff = (c >> 3) * (D_ * 2) + (4 * G + (c & 3)) * 16;
  const u32x4 qsrd = {(unsigned)(uint64_t)q, (unsigned)((uint64_t)q >> 32) & 0xffffu, (unsigned)(B_ * HQ * D_ * 2), 0x00020000u};
  asm volatile("s_waitcnt vmcnt(0)" : "+v"(lenv), "+v"(btv));
  const unsigned phys0 = rdl(btv, 0), phys1 = rdl(btv, 1);
  const int len0 = (int)rdl((unsigned)lenv, b);
  const int len_b = (len0 <= 0) ? NBPS * PAGE : min(len0, NBPS * PAGE);
  const unsigned lzb = (unsigned)len_b | ((len0 <= 0) ? 0x10000u : 0u);

  KTile K; VTile V;
  unsigned dc = phys0 << 11 | (lzb >> 16) << 10 | (unsigned)b << 5 | (unsigned)max(min(len_b - wave * PAGE, PAGE), 0);
  dc = __builtin_amdgcn_readfirstlane(dc);
  unsigned d1 = phys1 << 11 | (lzb >> 16) << 10 | (unsigned)b << 5 | (unsigned)max(min(len_b - (wave + W) * PAGE, PAGE), 0);
  issue_k<true>(K, kc, qsrd, dc, h, koff, qoff);
  issue_v<true>(V, vc, dc, voff);
  __builtin_amdgcn_sched_barrier(0);
  STAMP(1);

  const uint64_t wsl = (uint64_t)ws + WS_SLOTS + (uint64_t)h * (NSLOT * SLOT_BYTES);
  const u32x4 wsrd = {(unsigned)wsl, (unsigned)(wsl >> 32) & 0xffffu, (unsigned)(NSLOT * SLOT_BYTES), 0x00020000u};
  const unsigned zf = (lenv <= 0) ? 1u : 0u;
  const unsigned lz = zf ? (unsigned)(NBPS * PAGE) : (unsigned)min(lenv, NBPS * PAGE);

  const unsigned nv = (lz + PAGE - 1) >> 4;
  const unsigned T = rdl(scan32(nv, lane), 31);
  const unsigned r = (T + 127) >> 7, R = 4 * r;
  const unsigned ov = min(nv, R), sv = nv - ov, dv = R - ov;
  const unsigned inc = scan32(sv | dv << 16, lane);
  const unsigned SEv = inc & 0xffff, SPv = SEv - sv, DPv = (inc >> 16) - dv;
  const unsigned S_tot = rdl(SEv, 31);
  const unsigned o_b = rdl(ov, b), s_b = rdl(sv, b);
  const int o_w = (int)(o_b + 3 - wave) >> 2;
  int nf = 0; unsigned x0 = 0;
  {
    const unsigned pre = (wave > 0 ? (o_b + 3) >> 2 : 0u) + (wave > 1 ? (o_b + 2) >> 2 : 0u) + (wave > 2 ? (o_b + 1) >> 2 : 0u);
    x0 = rdl(DPv, b) + wave * r - pre;
    const unsigned x1 = min(x0 + r - (unsigned)o_w, S_tot);
    nf = (x1 > x0) ? (int)(x1 - x0) : 0;
  }
  const int nu = nf + max(o_w, 2);
  d1 = __builtin_amdgcn_readfirstlane(o_w >= 2 ? d1 : (d1 & ~31u));
  unsigned desc, ph, ev;
  {
    unsigned seq = b, page = wave + 4 * (lane - nf), info = lzb;
    bool live = lane > nf + 1 && lane < nu;
    if (nf > 0) {
      const unsigned x = x0 + lane - 2;
      unsigned cnt = 0;
#pragma unroll
      for (int j = 0; j < 31; ++j) cnt += (rdl(SEv, j) <= x) ? 1u : 0u;
      const unsigned pk = __shfl(SPv << 17 | zf << 16 | lz, (int)cnt);
      if (lane >= 2 && lane <= nf + 1) { seq = cnt; page = R + x - (pk >> 17); info = pk & 0x1ffff; live = true; }
    }
    page = live ? page : 0u;
    asm volatile("global_load_dword %0, %2, %3\n\tglobal_load_dword %1, %4, %5"
                 : "=&v"(ph), "=&v"(ev) : "v"((seq * NBPS + page) * 4), "s"(bt), "v"((h * 32 + (lane & 31)) * 4), "s"(ws) : "memory");
    const int rem = live ? min((int)(info & 0xffff) - (int)page * PAGE, PAGE) : 0;
    desc = (info >> 16) << 10 | seq << 5 | (unsigned)rem;
  }
  unsigned long long im0 = 0, im1 = 0;
  if (s_b > 0) {
    const unsigned sp = rdl(SPv, b), se = sp + s_b;
    bool ovl[2];
#pragma unroll
    for (int p = 0; p < 2; ++p) {
      const int g = lane + 64 * p, w2 = g & 3;
      const unsigned pk = __shfl(DPv << 8 | ov, g >> 2);
      const unsigned o2 = pk & 255;
      const unsigned pre = (w2 > 0 ? (o2 + 3) >> 2 : 0u) + (w2 > 1 ? (o2 + 2) >> 2 : 0u) + (w2 > 2 ? (o2 + 1) >> 2 : 0u);
      const unsigned a0 = (pk >> 8) + w2 * r - pre, a1 = a0 + r - ((o2 + 3 - w2) >> 2);
      ovl[p] = max(a0, sp) < min(a1, se);
    }
    const unsigned long long m0 = __ballot(ovl[0]), m1 = __ballot(ovl[1]);
    const unsigned rk0 = __builtin_amdgcn_mbcnt_hi((unsigned)(m0 >> 32), __builtin_amdgcn_mbcnt_lo((unsigned)m0, 0u));
    const unsigned rk1 = __builtin_amdgcn_mbcnt_hi((unsigned)(m1 >> 32), __builtin_amdgcn_mbcnt_lo((unsigned)m1, (unsigned)__popcll(m0)));
    im0 = __ballot(ovl[0] && (int)(rk0 & 3) == wave);
    im1 = __ballot(ovl[1] && (int)(rk1 & 3) == wave);
  }
  const int i_imp = nu - 1 - (__popcll(im0) + __popcll(im1));

  unsigned pm[4];
#pragma unroll
  for (int i = 0; i < 4; ++i) pm[i] = (((lane & 7) >> 1) == i) ? ((lane & 1) ? 0xffff0000u : 0x0000ffffu) : 0u;
  float m = M_INIT, l = 0.f, om = M_INIT, ol = 0.f;
  f32x4 acc0 = {0.f, 0.f, 0.f, 0.f}, acc1 = acc0, oacc0 = acc0, oacc1 = acc0;

  auto merge_imp = [&](Imp& I, unsigned soff) {
    const unsigned my_tag = (rdl(ev, b) + 1u) << 5 | (unsigned)b;
    for (int spin = 0;; ++spin) {
      bool ok = true;
#pragma unroll
      for (int k = 0; k < 5; ++k) ok = ok && I.r[k][1] == my_tag && I.r[k][3] == my_tag;
      if (__all(ok) || spin > (1 << 18)) break;
      __builtin_amdgcn_s_sleep(8);
      issue_imp(I, wsrd, soff, lane * 16);
      wait_imp<0>(I);
    }
    const float m2 = __uint_as_float(I.r[4][0]), l2 = __uint_as_float(I.r[4][2]);
    const float mn = fmaxf(m, m2);
    const float a = __builtin_amdgcn_exp2f(m - mn), a2 = __builtin_amdgcn_exp2f(m2 - mn);
    m = mn;
    l = l * a + l2 * a2;
    const float A = to_acc_layout(a, lane), A2 = to_acc_layout(a2, lane);
    const f32x4 b0 = {__uint_as_float(I.r[0][0]), __uint_as_float(I.r[0][2]), __uint_as_float(I.r[1][0]), __uint_as_float(I.r[1][2])};
    const f32x4 b1 = {__uint_as_float(I.r[2][0]), __uint_as_float(I.r[2][2]), __uint_as_float(I.r[3][0]), __uint_as_float(I.r[3][2])};
    acc0 = acc0 * A + b0 * A2;
    acc1 = acc1 * A + b1 * A2;
  };
  auto next_imp = [&]() -> unsigned {
    int g;
    if (im0) { g = __builtin_ctzll(im0); im0 &= im0 - 1; } else { g = 64 + __builtin_ctzll(im1); im1 &= im1 - 1; }
    return (unsigned)(g + b) * SLOT_BYTES;
  };

  STAMP(2);
  auto step = [&](auto IMPORT, int i) {
    wait_k<8>(K);
    asm volatile("" : "+v"(ph));
    const unsigned dn = (i == 0) ? d1 : (rdl(desc, i + 1) | rdl(ph, i + 1) << 11);
    float s[4];
#pragma unroll
    for (int u = 0; u < 4; ++u) {
      f32x4 d = {0.f, 0.f, 0.f, 0.f};
      d = mfma(K.r[2 * u], K.q[0], d);
      d = mfma(K.r[2 * u + 1], K.q[1], d);
      s[u] = (dpp<0x00>(d[0]) + dpp<0x55>(d[1])) + (dpp<0xAA>(d[2]) + dpp<0xFF>(d[3]));
    }
    issue_k<false>(K, kc, qsrd, dn, h, koff, qoff);
    const int rem = dc & 31;
    if (rem < PAGE) {
#pragma unroll
      for (int u = 0; u < 4; ++u) s[u] = (4 * u + G < rem) ? s[u] : -INFINITY;
    }
    float mx = fmaxf(fmaxf(s[0], s[1]), fmaxf(s[2], s[3]));
    mx = xrow_max(mx) * C2;
    if (__any(mx > m)) {
      const float mn = fmaxf(m, mx);
      const float al = __builtin_amdgcn_exp2f(m - mn);
      m = mn;
      l *= al;
      const float aa = to_acc_layout(al, lane);
      acc0 *= aa; acc1 *= aa;
    }
    u32x4 a[4];
#pragma unroll
    for (int u = 0; u < 4; ++u) {
      const float p = __builtin_amdgcn_exp2f(fmaf(s[u], C2, -m));
      l += p;
      const unsigned pk = cvtpk(p, p);
      a[u] = u32x4{pk & pm[0], pk & pm[1], pk & pm[2], pk & pm[3]};
    }
    wait_v<10>(V);
    asm volatile("" : "+v"(ev));
#pragma unroll
    for (int u = 0; u < 4; ++u) {
      acc0 = mfma(a[u], V.r[2 * u], acc0);
      acc1 = mfma(a[u], V.r[2 * u + 1], acc1);
    }
    if constexpr (!decltype(IMPORT)::value) {
    if (((dn ^ dc) >> 5) & 31) {
      const unsigned sc = (dc >> 5) & 31, sn = (dn >> 5) & 31;
      if (sc == (unsigned)b) { om = m; ol = l; oacc0 = acc0; oacc1 = acc1; }
      else {
        const unsigned tag = (rdl(ev, sc) + 1u) << 5 | sc;
        const unsigned so = (unsigned)(4 * b + wave + sc) * SLOT_BYTES;
        auto rs = __builtin_amdgcn_make_buffer_rsrc((void*)wsl, 0, NSLOT * SLOT_BYTES, 0x00020000);
        __builtin_amdgcn_raw_buffer_store_b128(u32x4{__float_as_uint(acc0[0]), tag, __float_as_uint(acc0[1]), tag}, rs, lane * 16, so, 16);
        __builtin_amdgcn_raw_buffer_store_b128(u32x4{__float_as_uint(acc0[2]), tag, __float_as_uint(acc0[3]), tag}, rs, lane * 16 + 1024, so, 16);
        __builtin_amdgcn_raw_buffer_store_b128(u32x4{__float_as_uint(acc1[0]), tag, __float_as_uint(acc1[1]), tag}, rs, lane * 16 + 2048, so, 16);
        __builtin_amdgcn_raw_buffer_store_b128(u32x4{__float_as_uint(acc1[2]), tag, __float_as_uint(acc1[3]), tag}, rs, lane * 16 + 3072, so, 16);
        __builtin_amdgcn_raw_buffer_store_b128(u32x4{__float_as_uint(m), tag, __float_as_uint(l), tag}, rs, lane * 16 + 4096, so, 16);
      }
      if (sn == (unsigned)b) { m = om; l = ol; acc0 = oacc0; acc1 = oacc1; }
      else { m = M_INIT; l = 0.f; acc0 = f32x4{0.f, 0.f, 0.f, 0.f}; acc1 = acc0; }
    }
      issue_v<false>(V, vc, dn, voff);
    } else {
      Imp I;
      const bool have = (im0 | im1) != 0;
      const unsigned so = have ? next_imp() : 0x40000000u;
      issue_imp(I, wsrd, so, lane * 16);
      issue_v<false>(V, vc, dn, voff);
      wait_imp<8>(I);
      if (have) merge_imp(I, so);
    }
    dc = dn;
  };
  {
    const int na = (im0 | im1) ? max(i_imp, 0) : nu;
    int i = 0;
    for (; i < na; ++i) step(std::false_type{}, i);
    for (; i < nu; ++i) step(std::true_type{}, i);
  }
  while (im0 | im1) {
    Imp I;
    const unsigned so = next_imp();
    issue_imp(I, wsrd, so, lane * 16);
    wait_imp<0>(I);
    merge_imp(I, so);
  }
  wait_k<0>(K); wait_v<0>(V);
  STAMP(3);

  l = xrow_sum(l);
  constexpr int REC = 2 * D_ + 4;
  float* rec = lds + wave * REC;
  {
    const int qq = lane >> 5, d0 = 8 * c + 4 * (G & 1);
    *reinterpret_cast<f32x4*>(rec + 4 + qq * D_ + d0) = acc0;
    *reinterpret_cast<f32x4*>(rec + 4 + qq * D_ + 128 + d0) = acc1;
    if (lane == 0 || lane == 8) { rec[lane >> 3] = m; rec[2 + (lane >> 3)] = l; }
  }
  __syncthreads();
  STAMP(4);
  {
    const int qq = tid >> 7, d0 = (tid & 127) * 2;
    float M = M_INIT;
#pragma unroll
    for (int w = 0; w < W; ++w) M = fmaxf(M, lds[w * REC + qq]);
    float L = 0.f, o0 = 0.f, o1 = 0.f;
#pragma unroll
    for (int w = 0; w < W; ++w) {
      const float* rr = lds + w * REC;
      const float wt = __builtin_amdgcn_exp2f(rr[qq] - M);
      const float2 a = *reinterpret_cast<const float2*>(rr + 4 + qq * D_ + d0);
      L = fmaf(wt, rr[2 + qq], L); o0 = fmaf(wt, a.x, o0); o1 = fmaf(wt, a.y, o1);
    }
    const float inv = 1.f / L;
    *reinterpret_cast<unsigned*>(out + ((size_t)b * HQ + h * 2 + qq) * D_ + d0) = cvtpk(o0 * inv, o1 * inv);
  }
  if (s_b > 0 && tid == 0)
    __hip_atomic_store(reinterpret_cast<unsigned*>(ws) + h * 32 + b, rdl(ev, b) + 1u, __ATOMIC_RELAXED, __HIP_MEMORY_SCOPE_AGENT);
}

extern "C" void kernel_launch(void* const* d_in, const int* in_sizes, int n_in, void* d_out, int out_size, void* d_ws,
                              size_t ws_size, hipStream_t stream) {
  (void)hipFuncSetAttribute((const void*)attn_kernel, hipFuncAttributeMaxDynamicSharedMemorySize, LDS_BYTES);
  attn_kernel<<<B_ * HKV, W * 64, LDS_BYTES, stream>>>((const unsigned char*)d_in[0], (const unsigned char*)d_in[1],
                                               (const unsigned char*)d_in[2], (const int*)d_in[3], (const int*)d_in[4],
                                               (unsigned short*)d_out, (unsigned char*)d_ws, (unsigned)(((size_t)in_sizes[1] * 2) >> 21)
                                               );
}
```

```cpp
#include <hip/hip_runtime.h>
#include <hip/hip_bf16.h>
#include <cstdint>

constexpr int B_ = 32, HQ = 16, HKV = 8, D_ = 256, PAGE = 16, NBPS = 128;
constexpr int W = 4;
constexpr int TOK_BYTES = HKV * D_ * 2;
constexpr int SLOT_BYTES = 2048 + 128, NSLOT = 64;
constexpr int WS_FLAGS = 1024, WS_SLOTS = 4096;
constexpr int WS_PAIR = 512 * 1024;
constexpr unsigned FLAG_X = 0x5bd1e995u;
constexpr int NPRE = 4;
constexpr int MAXU = 40;
constexpr int NSTG = 8;
constexpr int NB = 4;
constexpr int LDS_BYTES = 116 * 1024;
constexpr double PACE_BW = 7.30e12;
constexpr unsigned ROW_T = (unsigned)(4096.0 * (256.0 * W * 512.0) / PACE_BW * 1.0e8);
constexpr float M_INIT = -3.0e38f;

using bf16x8 = __attribute__((ext_vector_type(8))) __bf16;
using f32x4 = __attribute__((ext_vector_type(4))) float;
using u32x4 = __attribute__((ext_vector_type(4))) unsigned;

template <int CTRL> __device__ __forceinline__ float dpp(float x) {
  return __builtin_bit_cast(float, __builtin_amdgcn_mov_dpp(__builtin_bit_cast(int, x), CTRL, 0xf, 0xf, true));
}
__device__ __forceinline__ float xrow_max(float x) {
  auto s = __builtin_amdgcn_permlane16_swap(__float_as_uint(x), __float_as_uint(x), false, false);
  x = fmaxf(__uint_as_float(s[0]), __uint_as_float(s[1]));
  auto t = __builtin_amdgcn_permlane32_swap(__float_as_uint(x), __float_as_uint(x), false, false);
  return fmaxf(__uint_as_float(t[0]), __uint_as_float(t[1]));
}
__device__ __forceinline__ float xrow_sum(float x) {
  auto s = __builtin_amdgcn_permlane16_swap(__float_as_uint(x), __float_as_uint(x), false, false);
  x = __uint_as_float(s[0]) + __uint_as_float(s[1]);
  auto t = __builtin_amdgcn_permlane32_swap(__float_as_uint(x), __float_as_uint(x), false, false);
  return __uint_as_float(t[0]) + __uint_as_float(t[1]);
}
__device__ __forceinline__ unsigned cvtpk(float lo, float hi) {
  typedef __attribute__((ext_vector_type(2))) __bf16 bf16x2_t;
  typedef __attribute__((ext_vector_type(2))) float f32x2_t;
  return __builtin_bit_cast(unsigned, __builtin_convertvector(f32x2_t{lo, hi}, bf16x2_t));
}
__device__ __forceinline__ f32x4 mfma(u32x4 a, u32x4 b, f32x4 c) {
  return __builtin_amdgcn_mfma_f32_16x16x32_bf16(__builtin_bit_cast(bf16x8, a), __builtin_bit_cast(bf16x8, b), c, 0, 0, 0);
}
__device__ __forceinline__ unsigned rdl(unsigned x, int l) { return (unsigned)__builtin_amdgcn_readlane((int)x, l); }
__device__ __forceinline__ unsigned scan32(unsigned x, int lane) {
  x += (unsigned)__builtin_amdgcn_update_dpp(0, (int)x, 0x111, 0xf, 0xf, false);
  x += (unsigned)__builtin_amdgcn_update_dpp(0, (int)x, 0x112, 0xf, 0xf, false);
  x += (unsigned)__builtin_amdgcn_update_dpp(0, (int)x, 0x114, 0xf, 0xf, false);
  x += (unsigned)__builtin_amdgcn_update_dpp(0, (int)x, 0x118, 0xf, 0xf, false);
  const unsigned t = rdl(x, 15);
  return (lane & 16) ? x + t : x;
}
__device__ __forceinline__ float to_acc_layout(float a, int lane) {
  const float a0 = __builtin_bit_cast(float, __builtin_amdgcn_readlane(__builtin_bit_cast(int, a), 0));
  const float a1 = __builtin_bit_cast(float, __builtin_amdgcn_readlane(__builtin_bit_cast(int, a), 8));
  return (lane >= 32) ? a1 : a0;
}

__device__ __forceinline__ void merge_state(float& m, float& l, f32x4& acc0, f32x4& acc1, float m2, float l2, f32x4 b0, f32x4 b1, int lane) {
  const float mn = fmaxf(m, m2);
  const float a = __builtin_amdgcn_exp2f(m - mn), a2 = __builtin_amdgcn_exp2f(m2 - mn);
  m = mn;
  l = l * a + l2 * a2;
  const float A = to_acc_layout(a, lane), A2 = to_acc_layout(a2, lane);
  acc0 = acc0 * A + b0 * A2;
  acc1 = acc1 * A + b1 * A2;
}
#define LAS __attribute__((address_space(3)))
typedef volatile LAS unsigned* lds_u32p;
typedef volatile LAS float* lds_f32p;
typedef volatile LAS f32x4* lds_f32x4p;
struct Tile { u32x4 r[8]; u32x4 q[2]; };

#define LOADS8                                                           \
  "buffer_load_dwordx4 %0, %[vo], %[srd], 0 offen nt\n\t"                \
  "buffer_load_dwordx4 %1, %[vo], %[srd], 0 offen offset:256 nt\n\t"     \
  "buffer_load_dwordx4 %2, %[vo], %[srd], %[s1] offen nt\n\t"            \
  "buffer_load_dwordx4 %3, %[vo], %[srd], %[s1] offen offset:256 nt\n\t" \
  "buffer_load_dwordx4 %4, %[vo], %[srd], %[s2] offen nt\n\t"            \
  "buffer_load_dwordx4 %5, %[vo], %[srd], %[s2] offen offset:256 nt\n\t" \
  "buffer_load_dwordx4 %6, %[vo], %[srd], %[s3] offen nt\n\t"            \
  "buffer_load_dwordx4 %7, %[vo], %[srd], %[s3] offen offset:256 nt"

template <bool FIRST>
__device__ __forceinline__ void issue_q(Tile& T, const unsigned char* base, unsigned phys, unsigned rows, int off, u32x4 qsrd, unsigned qs,
                                        int qoff) {
  const uint64_t a = (uint64_t)base + ((uint64_t)phys << 16);
  const u32x4 srd = {(unsigned)a, (unsigned)(a >> 32) & 0xffffu, rows << 12, 0x00020000u};
#define KASM(CON)                                                                                                     \
  asm volatile("s_nop 4\n\t"                                                                                          \
               "buffer_load_dwordx4 %8, %[qo], %[qsrd], %[qs] offen\n\t"                                              \
               "buffer_load_dwordx4 %9, %[qo], %[qsrd], %[qs] offen offset:256\n\t" LOADS8                            \
               : CON(T.r[0]), CON(T.r[1]), CON(T.r[2]), CON(T.r[3]), CON(T.r[4]), CON(T.r[5]), CON(T.r[6]), CON(T.r[7]), \
                 CON(T.q[0]), CON(T.q[1])                                                                             \
               : [vo] "v"(off), [srd] "s"(srd), [s1] "s"(4 * TOK_BYTES), [s2] "s"(8 * TOK_BYTES),                     \
                 [s3] "s"(12 * TOK_BYTES), [qo] "v"(qoff), [qsrd] "s"(qsrd), [qs] "s"(qs)                             \
               : "memory")
  if constexpr (FIRST) KASM("=&v"); else KASM("+v");
#undef KASM
}
__device__ __forceinline__ void issue_v(Tile& T, const unsigned char* vc, unsigned d, int voff) {
  const uint64_t a = (uint64_t)vc + ((uint64_t)(d >> 11) << 16);
  const u32x4 srd = {(unsigned)a, (unsigned)(a >> 32) & 0xffffu, (d & 31u) << 12, 0x00020000u};
  asm volatile("s_nop 4\n\t" LOADS8
               : "+v"(T.r[0]), "+v"(T.r[1]), "+v"(T.r[2]), "+v"(T.r[3]), "+v"(T.r[4]), "+v"(T.r[5]), "+v"(T.r[6]), "+v"(T.r[7])
               : [vo] "v"(voff), [srd] "s"(srd), [s1] "s"(4 * TOK_BYTES), [s2] "s"(8 * TOK_BYTES), [s3] "s"(12 * TOK_BYTES)
               : "memory");
}
__device__ __forceinline__ void wait_tile(Tile& T) {
  asm volatile("s_waitcnt vmcnt(0)"
               : "+v"(T.r[0]), "+v"(T.r[1]), "+v"(T.r[2]), "+v"(T.r[3]), "+v"(T.r[4]), "+v"(T.r[5]), "+v"(T.r[6]), "+v"(T.r[7]),
                 "+v"(T.q[0]), "+v"(T.q[1]));
}
__global__ void __launch_bounds__((W + 1) * 64, 2)
attn_kernel(const unsigned char* __restrict__ q_, const unsigned char* __restrict__ kc_, const unsigned char* __restrict__ vc_,
            const int* __restrict__ bt_, const int* __restrict__ lens_, unsigned short* __restrict__ out_, unsigned char* ws_) {
  extern __shared__ __attribute__((aligned(16))) float lds[];
  unsigned pick;
  asm volatile("s_getreg_b32 %0, hwreg(HW_REG_XCC_ID)" : "=s"(pick));
  pick &= 1u;
  constexpr float C2 = 0.0625f * 1.4426950408889634f;

  constexpr int REC = 2 * D_ + 4;
  const lds_f32p stage = (lds_f32p)(lds + (W + 1) * REC);
  const lds_f32p sco = stage + W * NSTG * 640;
  const lds_u32p plan = (lds_u32p)(sco + W * MAXU * 32);
  const lds_u32p ctl = plan + W * 64;
#pragma clang loop unroll(disable)
  for (int pass = 0; pass < 2; ++pass) {
  int tid = threadIdx.x;
  unsigned blk = blockIdx.x;
  const unsigned char *q = q_, *kc = kc_, *vc = vc_;
  const int *bt = bt_, *lens = lens_;
  unsigned short* out = out_;
  unsigned char* ws = ws_;
  asm volatile("" : "+v"(tid), "+s"(blk));
  const int lane = tid & 63, b = blk >> 3;
  const int wave = __builtin_amdgcn_readfirstlane(tid >> 6);
  const int G = lane >> 4, c = lane & 15;
  const int h = ((int)((blk & 6u) | pick) - (int)((uintptr_t)kc >> 9)) & 7;
  float m = M_INIT, l = 0.f;
  f32x4 acc0 = {0.f, 0.f, 0.f, 0.f}, acc1 = acc0;

  if (wave == W) {
    __builtin_amdgcn_s_setprio(3);
    if (lane < 14 + W * NSTG) ctl[lane] = 0u;
    const int lenv = lens[lane & 31];
    unsigned char* const wsh = ws + ((size_t)h << 20);
    const unsigned zf = (lenv <= 0) ? 1u : 0u;
    const unsigned lz = zf ? (unsigned)(NBPS * PAGE) : (unsigned)min(lenv, NBPS * PAGE);
    const unsigned nv = (lz + PAGE - 1) >> 4;
    const unsigned T = rdl(scan32(nv, lane), 31);
    const unsigned r = max((T + 127) >> 7, (unsigned)NPRE), R = 4 * r;
    const unsigned ov = min(nv, R), sv = nv - ov, dv = R - ov;
    const unsigned inc = scan32(sv | dv << 16, lane);
    const unsigned SEv = inc & 0xffff, SPv = SEv - sv, DPv = (inc >> 16) - dv;
    const unsigned S_tot = rdl(SEv, 31);
    const unsigned s_b = rdl(sv, b), o_b = rdl(ov, b), lzb = rdl(lz | zf << 16, b);
    {
      unsigned x0 = rdl(DPv, b);
      unsigned dsc[W], pidx[W];
#pragma unroll
      for (int w = 0; w < W; ++w) {
        const int o_w = (int)(o_b + 3 - w) >> 2;
        const unsigned x1 = min(x0 + r - (unsigned)o_w, S_tot);
        const int nf = (x1 > x0) ? (int)(x1 - x0) : 0;
        const int nu = nf + max(o_w, NPRE);
        unsigned seq = b, page = w + 4 * (lane < NPRE ? lane : lane - nf), info = lzb;
        bool live = lane < NPRE ? lane < o_w : (lane >= nf + NPRE && lane < nu);
        if (nf > 0) {
          const unsigned x = x0 + lane - NPRE;
          unsigned cnt = 0;
#pragma unroll 1
          for (int j = 0; j < 31; ++j) cnt += (rdl(SEv, j) <= x) ? 1u : 0u;
          const unsigned pk = __shfl(SPv << 17 | zf << 16 | lz, (int)cnt);
          if (lane >= NPRE && lane < nf + NPRE) { seq = cnt; page = R + x - (pk >> 17); info = pk & 0x1ffff; live = true; }
        }
        page = live ? page : 0u;
        pidx[w] = seq * NBPS + page;
        const int rem = live ? min((int)(info & 0xffff) - (int)page * PAGE, PAGE) : 0;
        dsc[w] = (info >> 16) << 10 | seq << 5 | (unsigned)rem;
        if (lane == 63) dsc[w] = (unsigned)(nu | nf << 8);
        if (nf == 0 && lane == 0) ctl[8 + w] = 1u;
        x0 += r - (unsigned)o_w;
      }
      unsigned ph[W];
#pragma unroll
      for (int w = 0; w < W; ++w) ph[w] = (unsigned)bt[pidx[w]];
#pragma unroll
      for (int w = 0; w < W; ++w) plan[w * 64 + lane] = (lane == 63) ? dsc[w] : (dsc[w] | ph[w] << 11);
    }
    asm volatile("s_waitcnt lgkmcnt(0)\n\ts_barrier" ::: "memory");
    const unsigned ev = __hip_atomic_load(reinterpret_cast<const unsigned*>(wsh) + (lane & 31), __ATOMIC_RELAXED, __HIP_MEMORY_SCOPE_AGENT);
    bool drop = false;
    if (pass == 0) {
      unsigned* const pw = reinterpret_cast<unsigned*>(ws + ((size_t)(blk & 6u) << 20) + WS_PAIR + b * 128);
      unsigned long long* const cw = reinterpret_cast<unsigned long long*>(pw + 2);
      const unsigned ep = __builtin_amdgcn_readfirstlane(__hip_atomic_load(pw, __ATOMIC_RELAXED, __HIP_MEMORY_SCOPE_AGENT)) + 1u;
      const unsigned mine = ep << 1 | pick;
      if (lane == 0)
        __hip_atomic_store(cw + (blk & 1), (unsigned long long)(mine ^ FLAG_X) << 32 | mine, __ATOMIC_RELAXED, __HIP_MEMORY_SCOPE_AGENT);
#pragma unroll 1
      for (int spin = 0; spin < (1 << 16); ++spin) {
        __builtin_amdgcn_s_sleep(96);
        const unsigned long long o = __hip_atomic_load(cw + ((blk & 1) ^ 1), __ATOMIC_RELAXED, __HIP_MEMORY_SCOPE_AGENT);
        const unsigned lo = __builtin_amdgcn_readfirstlane((unsigned)o), hi = __builtin_amdgcn_readfirstlane((unsigned)(o >> 32));
        if (hi == (lo ^ FLAG_X) && ((lo ^ mine) >> 1) == 0) {
          drop = (lo == mine) && (blk & 1);
          if ((blk & 1) && lane == 0) __hip_atomic_store(pw, ep, __ATOMIC_RELAXED, __HIP_MEMORY_SCOPE_AGENT);
          break;
        }
      }
      if (drop && lane == 0) ctl[13 + W * NSTG] = 1u;
    }
    const uint64_t wsl = (uint64_t)wsh + WS_SLOTS;
    auto rs = __builtin_amdgcn_make_buffer_rsrc((void*)wsl, 0, NSLOT * SLOT_BYTES, 0x00020000);
    unsigned long long* const flags = reinterpret_cast<unsigned long long*>(wsh + WS_FLAGS);
    if (s_b > 0) {
      if (!drop) {
      const unsigned sp = rdl(SPv, b), se = sp + s_b;
      const bool mine = lane < 32 && max(DPv, sp) < min(DPv + dv, se);
      unsigned im = (unsigned)__ballot(mine);
      const unsigned tag = (rdl(ev, b) + 1u) << 5 | (unsigned)b;
      const unsigned long long want = (unsigned long long)(tag ^ FLAG_X) << 32 | tag;
      for (int spin = 0; ctl[12 + W * NSTG] < (r >> 1) && spin < (1 << 20); ++spin) __builtin_amdgcn_s_sleep(32);
#pragma unroll 1
      for (int spin = 0; spin < (1 << 16); ++spin) {
        unsigned long long f = want;
        if (mine) f = __hip_atomic_load(flags + lane + b, __ATOMIC_RELAXED, __HIP_MEMORY_SCOPE_AGENT);
        if (__all(f == want)) break;
        __builtin_amdgcn_s_sleep(64);
      }
      __builtin_amdgcn_fence(__ATOMIC_ACQUIRE, "wavefront");
      while (im) {
        unsigned so[NB]; int nb = 0;
#pragma unroll
        for (int j = 0; j < NB; ++j) {
          so[j] = 0x40000000u;
          if (im) { so[j] = (unsigned)(__builtin_ctz(im) + b) * SLOT_BYTES; im &= im - 1; nb = j + 1; }
        }
        u32x4 I[NB][3];
#pragma unroll
        for (int j = 0; j < NB; ++j) {
          I[j][0] = __builtin_amdgcn_raw_buffer_load_b128(rs, lane * 16, so[j], 16);
          I[j][1] = __builtin_amdgcn_raw_buffer_load_b128(rs, lane * 16 + 1024, so[j], 16);
          I[j][2] = __builtin_amdgcn_raw_buffer_load_b128(rs, 2048, so[j], 16);
        }
#pragma unroll
        for (int j = 0; j < NB; ++j) {
          if (j < nb) {
            const float m2 = __uint_as_float((lane & 8) ? I[j][2][1] : I[j][2][0]), l2 = __uint_as_float((lane & 8) ? I[j][2][3] : I[j][2][2]);
            merge_state(m, l, acc0, acc1, m2, l2, __builtin_bit_cast(f32x4, I[j][0]), __builtin_bit_cast(f32x4, I[j][1]), lane);
          }
        }
      }
      if (lane == 0)
        __hip_atomic_store(reinterpret_cast<unsigned*>(wsh) + b, rdl(ev, b) + 1u, __ATOMIC_RELAXED, __HIP_MEMORY_SCOPE_AGENT);
      }
    } else {
      unsigned cur = 32;
      unsigned pend = 32;
      auto raise_flag = [&]() {
        const unsigned tag = (rdl(ev, pend) + 1u) << 5 | pend;
        if (lane == 0)
          __hip_atomic_store(flags + b + pend, (unsigned long long)(tag ^ FLAG_X) << 32 | tag, __ATOMIC_RELAXED, __HIP_MEMORY_SCOPE_AGENT);
        pend = 32;
      };
      auto publish = [&]() {
        const float lt = xrow_sum(l);
        const u32x4 hdr = {rdl(__float_as_uint(m), 0), rdl(__float_as_uint(m), 8), rdl(__float_as_uint(lt), 0), rdl(__float_as_uint(lt), 8)};
        const unsigned so = (unsigned)(b + cur) * SLOT_BYTES;
        __builtin_amdgcn_raw_buffer_store_b128(__builtin_bit_cast(u32x4, acc0), rs, so + lane * 16, 0, 16);
        __builtin_amdgcn_raw_buffer_store_b128(__builtin_bit_cast(u32x4, acc1), rs, so + lane * 16 + 1024, 0, 16);
        __builtin_amdgcn_raw_buffer_store_b128(hdr, rs, (lane == 0) ? so + 2048 : 0x40000000u, 0, 16);
        if (pend != 32) {
          asm volatile("s_waitcnt vmcnt(3)" ::: "memory");
          raise_flag();
        }
        pend = cur;
        m = M_INIT; l = 0.f; acc0 = f32x4{0.f, 0.f, 0.f, 0.f}; acc1 = acc0;
      };
#pragma unroll 1
      for (int w = 0; w < W; ++w) {
        unsigned k = 0;
#pragma unroll 1
        for (int spin = 0; spin < (1 << 22); ++spin) {
          const unsigned fin = ctl[8 + w];
          const unsigned prod = ctl[w];
          if (prod == k) {
            if (fin) break;
            if (pend != 32) { asm volatile("s_waitcnt vmcnt(0)" ::: "memory"); raise_flag(); }
            __builtin_amdgcn_s_sleep(8);
            continue;
          }
          const unsigned sc = __builtin_amdgcn_readfirstlane(ctl[12 + NSTG * w + (k % NSTG)]);
          const lds_f32p sg = stage + (w * NSTG + (k % NSTG)) * 640;
          const f32x4 x0 = *(lds_f32x4p)(sg + lane * 4);
          const f32x4 x1 = *(lds_f32x4p)(sg + 256 + lane * 4);
          const float xm = sg[512 + lane * 2];
          const float xl = sg[513 + lane * 2];
          ++k;
          if (lane == 0) ctl[4 + w] = k;
          if (drop) continue;
          if (sc != cur) { if (cur != 32) publish(); cur = sc; }
          merge_state(m, l, acc0, acc1, xm, xl, x0, x1, lane);
        }
      }
      if (cur != 32) publish();
      if (pend != 32) { asm volatile("s_waitcnt vmcnt(0)" ::: "memory"); raise_flag(); }
    }
  } else {
    int len0; unsigned p0, p1, p2, p3;
    asm volatile("s_load_dword %0, %5, 0x0\n\ts_load_dword %1, %6, 0x0\n\ts_load_dword %2, %6, 0x10\n\t"
                 "s_load_dword %3, %6, 0x20\n\ts_load_dword %4, %6, 0x30"
                 : "=&s"(len0), "=&s"(p0), "=&s"(p1), "=&s"(p2), "=&s"(p3)
                 : "s"(lens + b), "s"(bt + b * NBPS + wave) : "memory");
    int koff = ((lane >> 2) & 3) * TOK_BYTES + h * (D_ * 2) + (4 * G + (lane & 3)) * 16;
    int voff = G * TOK_BYTES + h * (D_ * 2) + c * 16;
    int qoff = (c >> 3) * (D_ * 2) + (4 * G + (c & 3)) * 16;
    const u32x4 qsrd = {(unsigned)(uint64_t)q, (unsigned)((uint64_t)q >> 32) & 0xffffu, (unsigned)(B_ * HQ * D_ * 2), 0x00020000u};
    asm volatile("s_waitcnt lgkmcnt(0)" : "+s"(len0), "+s"(p0), "+s"(p1), "+s"(p2), "+s"(p3), "+v"(koff), "+v"(voff), "+v"(qoff));
    const int len_b = (len0 <= 0) ? NBPS * PAGE : len0;
    const unsigned own = ((len0 <= 0) ? 1024u : 0u) | (unsigned)b << 5;
    auto qs_of = [&](unsigned d) { return (((d >> 5) & 31u) * HQ + 2 * h) * (D_ * 2) + ((d & 1024u) << 20); };
    Tile T;
    unsigned dc;
    {
      int rem0 = len_b - wave * PAGE;
      rem0 = rem0 > PAGE ? PAGE : rem0;
      asm volatile("" : "+s"(rem0));
      rem0 = rem0 < 0 ? 0 : rem0;
      dc = p0 << 11 | own | (unsigned)rem0;
    }
    issue_q<true>(T, kc, dc >> 11, (dc & 1024u) ? 0u : (dc & 31u), koff, qsrd, qs_of(dc), qoff);
    __builtin_amdgcn_sched_barrier(0);
    const unsigned t0 = (unsigned)__builtin_amdgcn_s_memrealtime();
    unsigned due = ((dc & 1024u) ? 0u : (dc & 31u)) * ROW_T;
    auto pace = [&](unsigned now, unsigned rows) {
#pragma unroll 1
      for (int z = 0; z < 64 && (int)(((now - t0) << 12) - due) < 0; ++z) {
        __builtin_amdgcn_s_sleep(1);
        now = (unsigned)__builtin_amdgcn_s_memrealtime();
      }
      due += rows * ROW_T;
    };
    unsigned desc = ((lane & 3) == 1 ? p1 : (lane & 3) == 2 ? p2 : p3) << 11 | own |
                    (unsigned)max(min(len_b - (wave + W * (lane & 3)) * PAGE, PAGE), 0);
    const lds_f32p mysc = sco + wave * (MAXU * 32) + (lane >> 3) * 4;
    int nu = NPRE, nf = 0;
#pragma clang loop unroll(disable)
    for (int i = 0; i < nu; ++i) {
      if (i == NPRE - 1) {
        asm volatile("s_barrier" ::: "memory");
        desc = plan[wave * 64 + lane];
        const unsigned cnt = rdl(desc, 63);
        nu = cnt & 255; nf = cnt >> 8;
      }
      const bool lastk = i + 1 >= nu;
      const unsigned dn = rdl(desc, lastk ? (nf > 0 ? NPRE : 0) : i + 1);
      wait_tile(T);
      const unsigned now = (unsigned)__builtin_amdgcn_s_memrealtime();
      f32x4 d[4];
#pragma unroll
      for (int u = 0; u < 4; ++u) {
        d[u] = mfma(T.r[2 * u], T.q[0], f32x4{0.f, 0.f, 0.f, 0.f});
        d[u] = mfma(T.r[2 * u + 1], T.q[1], d[u]);
      }
      const unsigned rows = (!lastk && (dn & 1024u)) ? 0u : (dn & 31u);
      pace(now, rows);
      issue_q<false>(T, lastk ? vc : kc, dn >> 11, rows, lastk ? voff : koff, qsrd,
                     lastk ? 0x40000000u : qs_of(dn), qoff);
      f32x4 s;
#pragma unroll
      for (int u = 0; u < 4; ++u) s[u] = ((dpp<0x00>(d[u][0]) + dpp<0x55>(d[u][1])) + (dpp<0xAA>(d[u][2]) + dpp<0xFF>(d[u][3]))) * C2;
      const int rem = dc & 31;
      if (rem < PAGE) {
#pragma unroll
        for (int u = 0; u < 4; ++u) s[u] = (4 * u + G < rem) ? s[u] : -INFINITY;
      }
      if ((lane & 7) == 0) *(lds_f32x4p)(mysc + i * 32) = s;
      dc = dn;
    }
    unsigned pm[4];
#pragma unroll
    for (int i = 0; i < 4; ++i) pm[i] = (((lane & 7) >> 1) == i) ? ((lane & 1) ? 0xffff0000u : 0x0000ffffu) : 0u;
    unsigned npub = 0;
    int uc = nf > 0 ? NPRE : 0;
#pragma clang loop unroll(disable)
    for (int k = 0; k < nu; ++k) {
      const bool more = k + 1 < nu;
      const int k1 = k + 1;
      const int un = k1 < nf ? NPRE + k1 : (k1 - nf < NPRE ? k1 - nf : k1);
      const unsigned dn = rdl(desc, un);
      const f32x4 s = *(lds_f32x4p)(mysc + uc * 32);
      float mx = fmaxf(fmaxf(s[0], s[1]), fmaxf(s[2], s[3]));
      mx = xrow_max(mx);
      if (__any(mx > m)) {
        const float mn = fmaxf(m, mx);
        const float al = __builtin_amdgcn_exp2f(m - mn);
        m = mn;
        l *= al;
        const float aa = to_acc_layout(al, lane);
        acc0 *= aa; acc1 *= aa;
      }
      u32x4 a[4];
#pragma unroll
      for (int u = 0; u < 4; ++u) {
        const float p = __builtin_amdgcn_exp2f(s[u] - m);
        l += p;
        const unsigned pk = cvtpk(p, p);
        a[u] = u32x4{pk & pm[0], pk & pm[1], pk & pm[2], pk & pm[3]};
      }
      wait_tile(T);
      const unsigned now = (unsigned)__builtin_amdgcn_s_memrealtime();
#pragma unroll
      for (int u = 0; u < 4; ++u) {
        acc0 = mfma(a[u], T.r[2 * u], acc0);
        acc1 = mfma(a[u], T.r[2 * u + 1], acc1);
      }
      if (more) { pace(now, dn & 31u); issue_v(T, vc, dn, voff); }
      if (wave == 0 && lane == 0) ctl[12 + W * NSTG] = (unsigned)k;
      if (k < nf && (((dn ^ dc) >> 5) & 31)) {
        for (int spin = 0; ctl[4 + wave] + NSTG <= npub && spin < (1 << 22); ++spin) __builtin_amdgcn_s_sleep(2);
        const lds_f32p sg = stage + (wave * NSTG + (npub % NSTG)) * 640;
        *(lds_f32x4p)(sg + lane * 4) = acc0;
        *(lds_f32x4p)(sg + 256 + lane * 4) = acc1;
        sg[512 + lane * 2] = m;
        sg[513 + lane * 2] = l;
        if (lane == 0) ctl[12 + NSTG * wave + (npub % NSTG)] = (dc >> 5) & 31;
        asm volatile("s_waitcnt lgkmcnt(0)" ::: "memory");
        ++npub;
        if (lane == 0) { ctl[wave] = npub; if (k1 == nf) ctl[8 + wave] = 1u; }
        m = M_INIT; l = 0.f; acc0 = f32x4{0.f, 0.f, 0.f, 0.f}; acc1 = acc0;
      }
      dc = dn; uc = un;
    }
  }

  { const float lt = xrow_sum(l); l = (wave == W) ? l : lt; }
  float* rec = lds + wave * REC;
  {
    const int qq = lane >> 5, d0 = 8 * c + 4 * (G & 1);
    *reinterpret_cast<f32x4*>(rec + 4 + qq * D_ + d0) = acc0;
    *reinterpret_cast<f32x4*>(rec + 4 + qq * D_ + 128 + d0) = acc1;
    if (lane == 0 || lane == 8) { rec[lane >> 3] = m; rec[2 + (lane >> 3)] = l; }
  }
  asm volatile("s_waitcnt lgkmcnt(0)\n\ts_barrier" ::: "memory");
  if (__builtin_amdgcn_readfirstlane(ctl[13 + W * NSTG]) != 0u) {
    pick ^= 1u;
    asm volatile("s_waitcnt lgkmcnt(0)\n\ts_barrier" ::: "memory");
    continue;
  }
  if (tid < 256) {
    const int qq = tid >> 7, d0 = (tid & 127) * 2;
    float M = M_INIT;
#pragma unroll
    for (int w = 0; w <= W; ++w) M = fmaxf(M, lds[w * REC + qq]);
    float L = 0.f, o0 = 0.f, o1 = 0.f;
#pragma unroll
    for (int w = 0; w <= W; ++w) {
      const float* rr = lds + w * REC;
      const float wt = __builtin_amdgcn_exp2f(rr[qq] - M);
      const float2 a = *reinterpret_cast<const float2*>(rr + 4 + qq * D_ + d0);
      L = fmaf(wt, rr[2 + qq], L); o0 = fmaf(wt, a.x, o0); o1 = fmaf(wt, a.y, o1);
    }
    const float inv = 1.f / L;
    *reinterpret_cast<unsigned*>(out + ((size_t)b * HQ + h * 2 + qq) * D_ + d0) = cvtpk(o0 * inv, o1 * inv);
  }
  break;
  }
}

extern "C" void kernel_launch(void* const* d_in, const int* in_sizes, int n_in, void* d_out, int out_size, void* d_ws,
                              size_t ws_size, hipStream_t stream) {
  (void)hipFuncSetAttribute((const void*)attn_kernel, hipFuncAttributeMaxDynamicSharedMemorySize, LDS_BYTES);
  attn_kernel<<<B_ * HKV, (W + 1) * 64, LDS_BYTES, stream>>>((const unsigned char*)d_in[0], (const unsigned char*)d_in[1],
                                               (const unsigned char*)d_in[2], (const int*)d_in[3], (const int*)d_in[4],
                                               (unsigned short*)d_out, (unsigned char*)d_ws);
}
```
